```python
import torch

def _softmax(x, axis=-1):
    return torch.softmax(x, dim=axis)
def _gelu(x):
    return torch.nn.functional.gelu(x, approximate="tanh")
def _silu(x):
    return torch.nn.functional.silu(x)
def _sigmoid(x):
    return torch.sigmoid(x)
def _layernorm(x, axis=-1, eps=1e-5):
    m = x.mean(dim=axis, keepdim=True)
    v = x.var(dim=axis, keepdim=True, unbiased=False)
    return (x - m) / torch.sqrt(v + eps)
def _scatter_add(tgt, idx, src, axis=0):
    return tgt.clone().index_add_(axis, idx, src)
def _segment_sum(src, seg, n):
    out = torch.zeros((n,) + src.shape[1:], dtype=src.dtype)
    return out.index_add_(0, seg, src)
def _pad(x, pw):
    flat = tuple(v for pair in reversed(pw) for v in pair)
    return torch.nn.functional.pad(x, flat)

kernel_name = 'concat_heads__mha_block_cached__gqa_block_cached__decode'

def setup_inputs():

    _w = (lambda a: (lambda t: t.to(torch.bfloat16) if t.is_floating_point() else t)(torch.as_tensor(a)))
    def _A_setup():
        B, Sq, D = 1, 1, 640
        H, Sk, Dh = 32, 8192, 128
        return {
            "x": _w((lambda *s: torch.randn(*s, dtype=torch.bfloat16) * 0.8)(B, Sq, D)),
            "k_cache": _w((lambda *s: torch.randn(*s, dtype=torch.bfloat16) * 1.6)(B, H, Sk, Dh)),
            "v_cache": _w((lambda *s: torch.randn(*s, dtype=torch.bfloat16) * 0.2)(B, H, Sk, Dh)),
            "wq": _w((lambda *s: torch.randn(*s, dtype=torch.bfloat16) * 1.6)(D, H * Dh) * (D ** -0.5)),
            "wo": _w((lambda *s: torch.randn(*s, dtype=torch.bfloat16) * 0.2)(H * Dh, D) * ((H * Dh) ** -0.5)),
        }
    _A_in = _A_setup()

    def _B_setup():
        B, Sq, D = 1, 1, 640
        Hq, Hkv, Sk, Dh = 32, 32, 8192, 128
        assert Hq % Hkv == 0, (Hq, Hkv)
        return {
            "x": _w((lambda *s: torch.randn(*s, dtype=torch.bfloat16) * 0.8)(B, Sq, D)),
            "k_cache": _w((lambda *s: torch.randn(*s, dtype=torch.bfloat16) * 1.6)(B, Hkv, Sk, Dh)),
            "v_cache": _w((lambda *s: torch.randn(*s, dtype=torch.bfloat16) * 0.2)(B, Hkv, Sk, Dh)),
            "wq": _w((lambda *s: torch.randn(*s, dtype=torch.bfloat16) * 1.6)(D, Hq * Dh) * (D ** -0.5)),
            "wo": _w((lambda *s: torch.randn(*s, dtype=torch.bfloat16) * 0.2)(Hq * Dh, D) * ((Hq * Dh) ** -0.5)),
        }
    _B_in = _B_setup()

    return {
        "A_x": _A_in["x"],
        "A_k_cache": _A_in["k_cache"],
        "A_v_cache": _A_in["v_cache"],
        "A_wq": _A_in["wq"],
        "A_wo": _A_in["wo"],
        "B_x": _B_in["x"],
        "B_k_cache": _B_in["k_cache"],
        "B_v_cache": _B_in["v_cache"],
        "B_wq": _B_in["wq"],
        "B_wo": _B_in["wo"],
    }

def reference(A_x, A_k_cache, A_v_cache, A_wq, A_wo, B_x, B_k_cache, B_v_cache, B_wq, B_wo):
    def _A_ref(x, k_cache, v_cache, wq, wo):
        B, Sq, D = x.shape
        H, Dh = 32, 128
        q = (lambda x, *d: x.permute(*d))((x @ wq).reshape(B, Sq, H, Dh), 0, 2, 1, 3)
        scores = torch.einsum("bhqd,bhkd->bhqk", q, k_cache) * (Dh ** -0.5)
        p = _softmax(scores, axis=-1)
        o = torch.einsum("bhqk,bhkd->bhqd", p, v_cache)
        return (lambda x, *d: x.permute(*d))(o, 0, 2, 1, 3).reshape(B, Sq, H * Dh) @ wo

    def _B_ref(x, k_cache, v_cache, wq, wo):
        B, Sq, D = x.shape
        Hq, Dh = 32, 128
        Hkv = k_cache.shape[1]
        rep = Hq // Hkv
        q = (lambda x, *d: x.permute(*d))((x @ wq).reshape(B, Sq, Hq, Dh), 0, 2, 1, 3)
        k = (lambda x, n, axis: torch.repeat_interleave(x, n, dim=axis))(k_cache, rep, axis=1)
        v = (lambda x, n, axis: torch.repeat_interleave(x, n, dim=axis))(v_cache, rep, axis=1)
        scores = torch.einsum("bhqd,bhkd->bhqk", q, k) * (Dh ** -0.5)
        p = _softmax(scores, axis=-1)
        o = torch.einsum("bhqk,bhkd->bhqd", p, v)
        return (lambda x, *d: x.permute(*d))(o, 0, 2, 1, 3).reshape(B, Sq, Hq * Dh) @ wo

    a = _A_ref(A_x, A_k_cache, A_v_cache, A_wq, A_wo)
    b = _B_ref(B_x, B_k_cache, B_v_cache, B_wq, B_wo)
    return torch.concatenate([a, b], axis=-1)
```

```cpp
#include <hip/hip_runtime.h>
#include <hip/hip_bf16.h>
#include <stdint.h>

typedef unsigned v4u __attribute__((ext_vector_type(4)));
typedef unsigned v2u __attribute__((ext_vector_type(2)));
typedef float f32x4 __attribute__((ext_vector_type(4)));
typedef float f32x2 __attribute__((ext_vector_type(2)));
typedef __bf16 bf16x2_t __attribute__((ext_vector_type(2)));
typedef __bf16 bf16x8_t __attribute__((ext_vector_type(8)));
typedef unsigned long long u64;

#define KV_AUX 2
#define DEPTH 4

constexpr int DM = 640, NH = 32, SK = 8192, DH = 128;
constexpr int NSPLIT = 4, NW = 8, NTHR = NW * 64;
constexpr int KEYS_WG = SK / NSPLIT;
constexpr int NT = KEYS_WG / 16 / NW;
constexpr int NRED = 40;
constexpr int REC = 160;
constexpr int WS_EPOCH = 0;
constexpr int WS_SLAB = 4096;
constexpr int SLAB_BLK = NRED * 128 * REC;
constexpr int WS_BYTES = WS_SLAB + 2 * SLAB_BLK;
constexpr int AUX_SC1 = 16;
constexpr float CEXP = 0.08838834764831845f * 1.4426950408889634f;

constexpr int PSTR = 132;
constexpr int L_Q = 0;
constexpr int L_P = L_Q + NW * 128;
constexpr int L_Y = L_P + 32 * PSTR;
constexpr int L_ML = L_Y + 16 * 640;
constexpr int L_R = L_ML + 4;
constexpr int L_END = L_R + 32 * 16;

struct Blk {
  const void* x;
  const void* wq;
  const void* k;
  const void* v;
  const void* wo;
  void* out;
  void* ws;
  void* ws2;
};
struct Params { Blk b[2]; };

__device__ __forceinline__ float dot2(unsigned a, unsigned b, float c) {
  return __builtin_amdgcn_fdot2_f32_bf16(__builtin_bit_cast(bf16x2_t, a), __builtin_bit_cast(bf16x2_t, b), c, false);
}
template <int CTRL> __device__ __forceinline__ float dpp(float x) {
  return __builtin_bit_cast(float, __builtin_amdgcn_mov_dpp(__builtin_bit_cast(int, x), CTRL, 0xf, 0xf, true));
}
__device__ __forceinline__ float row_sum16(float x) {
  x += dpp<0x128>(x); x += dpp<0x124>(x); x += dpp<0x122>(x); x += dpp<0x121>(x); return x;
}
__device__ __forceinline__ float row_max16(float x) {
  x = fmaxf(x, dpp<0x128>(x)); x = fmaxf(x, dpp<0x124>(x)); x = fmaxf(x, dpp<0x122>(x)); x = fmaxf(x, dpp<0x121>(x)); return x;
}
__device__ __forceinline__ unsigned pack_bf16(float lo, float hi) {
  bf16x2_t r = {(__bf16)lo, (__bf16)hi};
  return __builtin_bit_cast(unsigned, r);
}
__device__ __forceinline__ float bf_lo(unsigned u) { return __builtin_bit_cast(float, u << 16); }
__device__ __forceinline__ float bf_hi(unsigned u) { return __builtin_bit_cast(float, u & 0xffff0000u); }
__device__ __forceinline__ f32x4 mfma16(v4u a, v4u b, f32x4 c) {
  return __builtin_amdgcn_mfma_f32_16x16x32_bf16(__builtin_bit_cast(bf16x8_t, a), __builtin_bit_cast(bf16x8_t, b), c, 0, 0, 0);
}
__device__ __forceinline__ __amdgpu_buffer_rsrc_t mk_rsrc(const void* p, int bytes) {
  return __builtin_amdgcn_make_buffer_rsrc((void*)p, 0, bytes, 0x00020000);
}
#define SCHED_FENCE() __builtin_amdgcn_sched_barrier(0)
#define WG_BARRIER() do { asm volatile("s_waitcnt lgkmcnt(0)" ::: "memory"); __builtin_amdgcn_s_barrier(); asm volatile("" ::: "memory"); } while (0)

__global__ void __launch_bounds__(NTHR, 2) attn_decode_fused(Params P) {
  __shared__ __attribute__((aligned(16))) float lds[L_END];
  const int b = blockIdx.x;
  const int hg = (b & 7) * 8 + (b >> 5);
  const int sp = (b >> 3) & 3;
  const int blk = hg >> 5, h = hg & 31;
  const int tid = threadIdx.x, lane = tid & 63, w = __builtin_amdgcn_readfirstlane(tid >> 6);
  const int g = lane >> 4, c = lane & 15;

  const Blk B = P.b[blk];
  const size_t kv_off = ((size_t)h * SK + (size_t)sp * KEYS_WG) * (DH * 2);
  const __amdgpu_buffer_rsrc_t rk = mk_rsrc((const char*)B.k + kv_off, KEYS_WG * DH * 2);
  const __amdgpu_buffer_rsrc_t rv = mk_rsrc((const char*)B.v + kv_off, KEYS_WG * DH * 2);
  const __amdgpu_buffer_rsrc_t rwq = mk_rsrc(B.wq, DM * NH * DH * 2);
  const __amdgpu_buffer_rsrc_t rwo = mk_rsrc((const char*)B.wo + (size_t)h * DH * DM * 2, DH * DM * 2);
  const __amdgpu_buffer_rsrc_t rx = mk_rsrc(B.x, DM * 2);
  const __amdgpu_buffer_rsrc_t rws = mk_rsrc(B.ws, WS_BYTES);

  constexpr int QR = DM / NW / 4;
  v2u xv[QR / 4];
#pragma unroll
  for (int i = 0; i < QR / 4; ++i) xv[i] = __builtin_amdgcn_raw_buffer_load_b64(rx, (w * (4 * QR) + g * QR) * 2 + 8 * i, 0, 0);
  v4u wq[QR];
  {
    const int vo = (g * QR) * (NH * DH * 2) + c * 16;
    const int so = (w * 4 * QR) * (NH * DH * 2) + h * (DH * 2);
#pragma unroll
    for (int u = 0; u < QR; ++u) wq[u] = __builtin_amdgcn_raw_buffer_load_b128(rwq, vo, so + u * (NH * DH * 2), 0);
  }
  int eoff = WS_EPOCH + blk * 256;
  asm volatile("" : "+v"(eoff));
  const v2u ep = __builtin_amdgcn_raw_buffer_load_b64(rws, eoff, 0, AUX_SC1);
  SCHED_FENCE();
  __builtin_amdgcn_s_barrier();
  SCHED_FENCE();
  v4u kr[DEPTH][4], vr[DEPTH][4];
  const int lo = lane * 16;
  const int wo_s = w * 4096;
#pragma unroll
  for (int i = 0; i < DEPTH; ++i) {
#pragma unroll
    for (int t = 0; t < 4; ++t) kr[i][t] = __builtin_amdgcn_raw_buffer_load_b128(rk, lo + t * 1024, wo_s + i * (NW * 4096), KV_AUX);
#pragma unroll
    for (int t = 0; t < 4; ++t) vr[i][t] = __builtin_amdgcn_raw_buffer_load_b128(rv, lo + t * 1024, wo_s + i * (NW * 4096), KV_AUX);
  }
  SCHED_FENCE();

  v4u msk;
  {
    const unsigned hm = (c & 1) ? 0xffff0000u : 0x0000ffffu;
    const int jj = (c & 7) >> 1;
    msk[0] = jj == 0 ? hm : 0u; msk[1] = jj == 1 ? hm : 0u; msk[2] = jj == 2 ? hm : 0u; msk[3] = jj == 3 ? hm : 0u;
  }
  unsigned qp[4];
  {
    f32x4 d0 = {0.f, 0.f, 0.f, 0.f}, d1 = {0.f, 0.f, 0.f, 0.f};
#pragma unroll
    for (int u = 0; u < QR; ++u) {
      const unsigned xw = xv[u >> 2][(u >> 1) & 1];
      const unsigned rep = (u & 1) ? ((xw & 0xffff0000u) | (xw >> 16)) : ((xw & 0xffffu) | (xw << 16));
      const v4u a = {rep & msk[0], rep & msk[1], rep & msk[2], rep & msk[3]};
      if (u & 1) d1 = mfma16(a, wq[u], d1); else d0 = mfma16(a, wq[u], d0);
    }
    d0 += d1;
    if (g < 2) *(f32x4*)&lds[L_Q + w * 128 + 8 * c + 4 * g] = d0;
    WG_BARRIER();
    f32x4 s0 = {0.f, 0.f, 0.f, 0.f}, s1 = {0.f, 0.f, 0.f, 0.f};
#pragma unroll
    for (int ww = 0; ww < NW; ++ww) {
      s0 += *(const f32x4*)&lds[L_Q + ww * 128 + 8 * c];
      s1 += *(const f32x4*)&lds[L_Q + ww * 128 + 8 * c + 4];
    }
    qp[0] = pack_bf16(s0[0], s0[1]); qp[1] = pack_bf16(s0[2], s0[3]);
    qp[2] = pack_bf16(s1[0], s1[1]); qp[3] = pack_bf16(s1[2], s1[3]);
  }
  SCHED_FENCE();

  constexpr int WR = DH / NW / 4;
  v4u wor[WR * 5];
  const int wo_v = (g * WR) * (DM * 2) + c * 16;
  const int wo_b = (w * 4 * WR) * (DM * 2);
  float m = -3.0e38f, l = 0.f;
  float acc[8];
#pragma unroll
  for (int j = 0; j < 8; ++j) acc[j] = 0.f;
#pragma unroll
  for (int i = 0; i < NT; ++i) {
    const int r = i % DEPTH;
    float x[4];
#pragma unroll
    for (int t = 0; t < 4; ++t) {
      float d = dot2(kr[r][t][0], qp[0], 0.f);
      d = dot2(kr[r][t][1], qp[1], d); d = dot2(kr[r][t][2], qp[2], d); d = dot2(kr[r][t][3], qp[3], d);
      x[t] = d;
    }
    SCHED_FENCE();
    if (i + DEPTH < NT) {
#pragma unroll
      for (int t = 0; t < 4; ++t) kr[r][t] = __builtin_amdgcn_raw_buffer_load_b128(rk, lo + t * 1024, wo_s + (i + DEPTH) * (NW * 4096), KV_AUX);
    } else {
      const int e = (i + DEPTH - NT) * 8;
#pragma unroll
      for (int t = 0; t < 4; ++t) if (e + t < WR * 5)
        wor[e + t] = __builtin_amdgcn_raw_buffer_load_b128(rwo, wo_v + ((e + t) % 5) * 256, wo_b + ((e + t) / 5) * (DM * 2), 0);
    }
    SCHED_FENCE();
#pragma unroll
    for (int t = 0; t < 4; ++t) x[t] = row_sum16(x[t]);
    const float mn = fmaxf(fmaxf(fmaxf(x[0], x[1]), fmaxf(x[2], x[3])), m);
    const float al = __builtin_amdgcn_exp2f((m - mn) * CEXP);
    const float mc = mn * CEXP;
    float p[4];
#pragma unroll
    for (int t = 0; t < 4; ++t) p[t] = __builtin_amdgcn_exp2f(fmaf(x[t], CEXP, -mc));
    l = fmaf(l, al, (p[0] + p[1]) + (p[2] + p[3]));
    m = mn;
#pragma unroll
    for (int j = 0; j < 8; ++j) acc[j] *= al;
#pragma unroll
    for (int t = 0; t < 4; ++t)
#pragma unroll
      for (int j = 0; j < 4; ++j) {
        acc[2 * j] = fmaf(p[t], bf_lo(vr[r][t][j]), acc[2 * j]);
        acc[2 * j + 1] = fmaf(p[t], bf_hi(vr[r][t][j]), acc[2 * j + 1]);
      }
    asm volatile("" : "+v"(acc[0]), "+v"(acc[1]), "+v"(acc[2]), "+v"(acc[3]), "+v"(acc[4]), "+v"(acc[5]), "+v"(acc[6]), "+v"(acc[7]), "+v"(l));
    SCHED_FENCE();
    if (i + DEPTH < NT) {
#pragma unroll
      for (int t = 0; t < 4; ++t) vr[r][t] = __builtin_amdgcn_raw_buffer_load_b128(rv, lo + t * 1024, wo_s + (i + DEPTH) * (NW * 4096), KV_AUX);
    } else {
      const int e = (i + DEPTH - NT) * 8 + 4;
#pragma unroll
      for (int t = 0; t < 4; ++t) if (e + t < WR * 5)
        wor[e + t] = __builtin_amdgcn_raw_buffer_load_b128(rwo, wo_v + ((e + t) % 5) * 256, wo_b + ((e + t) / 5) * (DM * 2), 0);
    }
    SCHED_FENCE();
  }

  {
    float* rec = &lds[L_P + (w * 4 + g) * PSTR];
    *(f32x4*)&rec[8 * c] = f32x4{acc[0], acc[1], acc[2], acc[3]};
    *(f32x4*)&rec[8 * c + 4] = f32x4{acc[4], acc[5], acc[6], acc[7]};
    if (c == 0) *(f32x2*)&rec[128] = f32x2{m, l};
  }
  WG_BARRIER();
  {
    const float* r0 = &lds[L_P + c * PSTR];
    const float* r1 = &lds[L_P + (c + 16) * PSTR];
    const f32x2 ml0 = *(const f32x2*)&r0[128], ml1 = *(const f32x2*)&r1[128];
    const f32x4 a0 = *(const f32x4*)&r0[w * 16 + 4 * g], a1 = *(const f32x4*)&r1[w * 16 + 4 * g];
    const float M = row_max16(fmaxf(ml0[0], ml1[0]));
    const float f0 = __builtin_amdgcn_exp2f((ml0[0] - M) * CEXP), f1 = __builtin_amdgcn_exp2f((ml1[0] - M) * CEXP);
    const float L = row_sum16(fmaf(f0, ml0[1], f1 * ml1[1]));
    if (tid == 0) *(f32x2*)&lds[L_ML] = f32x2{M, L};
    f32x4 y[5];
#pragma unroll
    for (int i = 0; i < 5; ++i) y[i] = f32x4{0.f, 0.f, 0.f, 0.f};
#pragma unroll
    for (int u = 0; u < WR; ++u) {
      const float a = row_sum16(fmaf(f0, a0[u], f1 * a1[u]));
      const unsigned hi2 = pack_bf16(a, a);
      const float rs = a - bf_hi(hi2);
      const unsigned lo2 = pack_bf16(rs, rs);
      const unsigned rep = c < 8 ? hi2 : lo2;
      const v4u af = {rep & msk[0], rep & msk[1], rep & msk[2], rep & msk[3]};
#pragma unroll
      for (int i = 0; i < 5; ++i) y[i] = mfma16(af, wor[u * 5 + i], y[i]);
    }
    float* yp = &lds[L_Y + (w * 2 + (g >> 1)) * 640 + 8 * c + 4 * (g & 1)];
#pragma unroll
    for (int i = 0; i < 5; ++i) *(f32x4*)&yp[128 * i] = y[i];
  }
  WG_BARRIER();
  const u64 e64 = ((u64)ep[1] << 32) | ep[0];
  const u64 t64 = e64 * 6364136223846793005ull + 1442695040888963407ull;
  const unsigned tagA = (unsigned)t64, tagB = (unsigned)(t64 >> 32);
  const int pidx = h * 4 + sp;
  if (tid < NRED * 10) {
    f32x2 val;
    int r, u;
    if (tid < 320) {
      r = tid >> 3; u = tid & 7;
      val = f32x2{0.f, 0.f};
#pragma unroll
      for (int j = 0; j < 16; ++j) val += *(const f32x2*)&lds[L_Y + j * 640 + 2 * tid];
    } else {
      r = (tid - 320) >> 1; u = 8 + (tid & 1);
      val = *(const f32x2*)&lds[L_ML];
    }
    const v4u st = {__float_as_uint(val[0]), tagA, __float_as_uint(val[1]), tagB};
    __builtin_amdgcn_raw_buffer_store_b128(st, rws, WS_SLAB + blk * SLAB_BLK + (r * 128 + pidx) * REC + u * 16, 0, AUX_SC1);
  }

  int red = -1;
  if (sp == 0) red = h; else if (sp == 1 && (h & 3) == 0) red = 32 + (h >> 2);
  if (red < 0) return;
  {
    const int base = WS_SLAB + blk * SLAB_BLK + (red * 128 + (tid >> 2)) * REC;
    const int o0 = base + (tid & 3) * 32, o1 = o0 + 16, o2 = base + 128;
    v4u u0, u1, u2;
    bool ok = false;
    const u64 t_start = __builtin_amdgcn_s_memrealtime();
    for (;;) {
      if (!ok) {
        u0 = __builtin_amdgcn_raw_buffer_load_b128(rws, o0, 0, AUX_SC1);
        u1 = __builtin_amdgcn_raw_buffer_load_b128(rws, o1, 0, AUX_SC1);
        u2 = __builtin_amdgcn_raw_buffer_load_b128(rws, o2, 0, AUX_SC1);
        ok = u0[1] == tagA && u0[3] == tagB && u1[1] == tagA && u1[3] == tagB && u2[1] == tagA && u2[3] == tagB;
      }
      asm volatile("" ::: "memory");
      if (__all(ok)) break;
      if (__builtin_amdgcn_s_memrealtime() - t_start > 200000000ull) break;
      __builtin_amdgcn_s_sleep(1);
    }
    const float mp = __uint_as_float(u2[0]), lp = __uint_as_float(u2[2]);
    float M = fmaxf(mp, dpp<0x128>(mp)); M = fmaxf(M, dpp<0x124>(M));
    const float f = __builtin_amdgcn_exp2f((mp - M) * CEXP);
    float L = f * lp; L += dpp<0x128>(L); L += dpp<0x124>(L);
    const float cf = f / L;
    f32x4 v = {__uint_as_float(u0[0]) * cf, __uint_as_float(u0[2]) * cf,
               __uint_as_float(u1[0]) * cf, __uint_as_float(u1[2]) * cf};
#pragma unroll
    for (int j = 0; j < 4; ++j) { v[j] += dpp<0x128>(v[j]); v[j] += dpp<0x124>(v[j]); }
    if (c < 4) *(f32x4*)&lds[L_R + (w * 4 + g) * 16 + 4 * c] = v;
    WG_BARRIER();
    if (w == 0) {
      float s = 0.f;
#pragma unroll
      for (int j = 0; j < 8; ++j) s += lds[L_R + (g * 8 + j) * 16 + c];
      s += __shfl_xor(s, 16);
      s += __shfl_xor(s, 32);
      if (lane < 16) ((__bf16*)B.out)[blk * DM + red * 16 + lane] = (__bf16)s;
      if (red == 0 && lane == 0) {
        const v2u ne = {tagA, tagB};
        __builtin_amdgcn_raw_buffer_store_b64(ne, rws, WS_EPOCH + blk * 256, 0, AUX_SC1);
      }
    }
  }
}

extern "C" void kernel_launch(void* const* d_in, const int* in_sizes, int n_in,
                              void* d_out, int out_size, void* d_ws, size_t ws_size,
                              hipStream_t stream) {
  Params P;
  for (int i = 0; i < 2; ++i) {
    P.b[i].x = d_in[5 * i]; P.b[i].k = d_in[5 * i + 1]; P.b[i].v = d_in[5 * i + 2];
    P.b[i].wq = d_in[5 * i + 3]; P.b[i].wo = d_in[5 * i + 4];
    P.b[i].out = d_out; P.b[i].ws = d_ws; P.b[i].ws2 = d_ws;
  }
  hipLaunchKernelGGL(attn_decode_fused, dim3(256), dim3(NTHR), 0, stream, P);
}
```

```cpp
#include <hip/hip_runtime.h>
#include <hip/hip_bf16.h>
#include <stdint.h>
#include <stdio.h>

typedef unsigned v4u __attribute__((ext_vector_type(4)));
typedef unsigned v2u __attribute__((ext_vector_type(2)));
typedef float f32x4 __attribute__((ext_vector_type(4)));
typedef float f32x2 __attribute__((ext_vector_type(2)));
typedef __bf16 bf16x2_t __attribute__((ext_vector_type(2)));
typedef __bf16 bf16x8_t __attribute__((ext_vector_type(8)));
typedef unsigned v16u __attribute__((ext_vector_type(16)));
typedef unsigned long long u64;

#define AUX_A 2
#define AUX_B 2
#define MAP 0
#define PROLOG 1
#define ROTATE 1
#define WQ_AUX 2
#define WO_AUX 0

constexpr int DM = 640, NH = 32, SK = 8192, DH = 128;
constexpr int NSPLIT = 4, NW = 8, NTHR = NW * 64;
constexpr int KEYS_WG = SK / NSPLIT;
constexpr int NSTEP = KEYS_WG / 64 / NW;
#define KVPAT "aijbpsdPcQS"
constexpr int kv_bit_(char r) { int i = 0; while (KVPAT[i] != r) ++i; return 1024 << i; }
constexpr int KB_a = kv_bit_('a'), KB_b = kv_bit_('b'), KB_c = kv_bit_('c'), KB_d = kv_bit_('d'), KB_i = kv_bit_('i'), KB_j = kv_bit_('j'),
              KB_Q = kv_bit_('Q'), KB_s = kv_bit_('s'), KB_S = kv_bit_('S'), KB_p = kv_bit_('p'), KB_P = kv_bit_('P');
constexpr int HEAD_BYTES = SK * DH * 2;
#define KV_STEP(it) (((it) & 1) * KB_s + (((it) >> 1) & 1) * KB_S)
constexpr int NRED = 64;
constexpr int WS_EPOCH = 0;
constexpr int WS_Q = 4096;
constexpr int WS_ML = WS_Q + 256 * 1024;
constexpr int WS_SLAB = WS_ML + 256 * 128;
constexpr int SLAB_BLK = NRED * 128 * 128;
constexpr int WS_BYTES = WS_SLAB + 2 * SLAB_BLK;
constexpr int OOB = 0x7ffffff0;
constexpr int AUX_SC1 = 16;
constexpr int POLL_MAX = 1 << 21;
constexpr float SCALE = 0.08838834764831845f, LOG2E = 1.4426950408889634f;

constexpr int L_A = 0;
constexpr int L_Q = L_A + 1024;
constexpr int L_QB = L_Q + NW * 128;
constexpr int L_ML = L_QB + 64;
constexpr int L_CNT = L_ML + 2 * NW;
constexpr int L_R = L_CNT + 4;
constexpr int L_END = L_R + 16 * NW + 64;

struct Blk {
  const void* x;
  const void* wq;
  const void* k;
  const void* v;
  const void* wo;
  void* out;
  void* ws;
  void* ws2;
};
struct Params { Blk b[2]; };

__device__ __forceinline__ float dot2(unsigned a, unsigned b, float c) {
  return __builtin_amdgcn_fdot2_f32_bf16(__builtin_bit_cast(bf16x2_t, a), __builtin_bit_cast(bf16x2_t, b), c, false);
}
template <int CTRL> __device__ __forceinline__ float dpp(float x) {
  return __builtin_bit_cast(float, __builtin_amdgcn_mov_dpp(__builtin_bit_cast(int, x), CTRL, 0xf, 0xf, true));
}
__device__ __forceinline__ float row_sum16(float x) {
  x += dpp<0x128>(x); x += dpp<0x124>(x); x += dpp<0x122>(x); x += dpp<0x121>(x); return x;
}
__device__ __forceinline__ float row_max16(float x) {
  x = fmaxf(x, dpp<0x128>(x)); x = fmaxf(x, dpp<0x124>(x)); x = fmaxf(x, dpp<0x122>(x)); x = fmaxf(x, dpp<0x121>(x)); return x;
}
__device__ __forceinline__ unsigned pack_bf16(float lo, float hi) {
  bf16x2_t r = {(__bf16)lo, (__bf16)hi};
  return __builtin_bit_cast(unsigned, r);
}
__device__ __forceinline__ float bf_lo(unsigned u) { return __builtin_bit_cast(float, u << 16); }
__device__ __forceinline__ float bf_hi(unsigned u) { return __builtin_bit_cast(float, u & 0xffff0000u); }
__device__ __forceinline__ unsigned split_bf16(float a) {
  const float hi = bf_lo(pack_bf16(a, a));
  return pack_bf16(hi, a - hi);
}
__device__ __forceinline__ float half_sum(float x) {
  const auto t = __builtin_amdgcn_permlane32_swap(__float_as_uint(x), __float_as_uint(x), false, false);
  return __uint_as_float(t[0]) + __uint_as_float(t[1]);
}
__device__ __forceinline__ float fold32(float a, float b) {
  const auto t = __builtin_amdgcn_permlane32_swap(__float_as_uint(a), __float_as_uint(b), false, false);
  return __uint_as_float(t[0]) + __uint_as_float(t[1]);
}
__device__ __forceinline__ float fold16(float a, float b) {
  const auto t = __builtin_amdgcn_permlane16_swap(__float_as_uint(a), __float_as_uint(b), false, false);
  return __uint_as_float(t[0]) + __uint_as_float(t[1]);
}
__device__ __forceinline__ float xrow_sum(float x) {
  const auto s = __builtin_amdgcn_permlane16_swap(__float_as_uint(x), __float_as_uint(x), false, false);
  return half_sum(__uint_as_float(s[0]) + __uint_as_float(s[1]));
}
__device__ __forceinline__ float xrow_max(float x) {
  const auto s = __builtin_amdgcn_permlane16_swap(__float_as_uint(x), __float_as_uint(x), false, false);
  x = fmaxf(__uint_as_float(s[0]), __uint_as_float(s[1]));
  const auto t = __builtin_amdgcn_permlane32_swap(__float_as_uint(x), __float_as_uint(x), false, false);
  return fmaxf(__uint_as_float(t[0]), __uint_as_float(t[1]));
}
__device__ __forceinline__ f32x4 mfma16(v4u a, v4u b, f32x4 c) {
  return __builtin_amdgcn_mfma_f32_16x16x32_bf16(__builtin_bit_cast(bf16x8_t, a), __builtin_bit_cast(bf16x8_t, b), c, 0, 0, 0);
}
__device__ __forceinline__ __amdgpu_buffer_rsrc_t mk_rsrc(const void* p, int bytes) {
  return __builtin_amdgcn_make_buffer_rsrc((void*)p, 0, bytes, 0x00020000);
}
#define STAMP(i)
#define STAMP_FLUSH()
#define STEP_SYNC 0
#define K_EARLY 8
#define WO_PRE 5
#define WO_PER 5
#define TOUCH 0
#define PRETOUCH 0
#define POLL 0
#define POLL_SLEEP 0
#define ABL 0
#define OUT_AUX 0
#define XD 0
#define XN 48
#define WO_AT 16
#define K_EARLY0 K_EARLY
#define XS 0
#define XM 0
constexpr int issued(int i) { return i + 16 + XD - (i >= XN ? XD : i < XS ? 0 : (i - XS) * XD / (XN - XS)) - (i < 48 ? ((i + 1) / 2 < XM ? (i + 1) / 2 : XM) : ((64 - i) / 2 < XM ? (64 - i) / 2 : XM)); }
#define CODEPF 128
#define QTOUCH 2
#define QP0 16
#define QP1 99
#define QP2 99
#define PUMP 0
#define PSLEEP 2
#define KEEP_H 31
#define LASTW 1
#define Y_AUX 16
#define P_AUX 16
#define PRE 2
#define PROBE 0
#define PROBE_AT(n) do { if (PROBE == (n)) { asm volatile("s_waitcnt vmcnt(0) lgkmcnt(0)" ::: "memory"); STAMP(1); } } while (0)
#define SCHED_FENCE() __builtin_amdgcn_sched_barrier(0)
#define WG_BARRIER() do { asm volatile("s_waitcnt lgkmcnt(0)" ::: "memory"); __builtin_amdgcn_s_barrier(); asm volatile("" ::: "memory"); } while (0)

template <int KV_AUX> __device__ __forceinline__ void attn_body(const Params& P, float* lds) {
  const int b = blockIdx.x;
  STAMP(0);
  unsigned cpf;
  {
    u64 pc; int len;
    asm volatile("s_getpc_b64 %0\n.Lattn_pf_from:\n\ts_mov_b32 %1, .Lattn_pf_to - .Lattn_pf_from" : "=s"(pc), "=s"(len));
    cpf = __builtin_amdgcn_raw_buffer_load_b32(mk_rsrc((const void*)pc, len < 0 ? 0 : len), threadIdx.x * CODEPF, 0, 0);
    SCHED_FENCE();
  }
  const int hg = (b & 7) * 8 + (b >> 5);
  const int sp = (b >> 3) & 3;
  const int blk = MAP ? hg & 1 : hg >> 5, h = MAP ? hg >> 1 : hg & 31;
  const int tid = threadIdx.x, lane = tid & 63, w = __builtin_amdgcn_readfirstlane(tid >> 6);
  const int g = lane >> 4, c = lane & 15;

  const Blk B = P.b[blk];
  const size_t kv_off = (size_t)h * HEAD_BYTES;
  const char* kbase = (const char*)B.k + kv_off;
  const char* vbase = (const char*)B.v + kv_off;
  const char* wobase = (const char*)B.wo + (size_t)h * DH * DM * 2;
  const __amdgpu_buffer_rsrc_t rk = mk_rsrc(kbase, HEAD_BYTES);
  const __amdgpu_buffer_rsrc_t rwq = mk_rsrc(B.wq, DM * NH * DH * 2);
  const __amdgpu_buffer_rsrc_t rwo = mk_rsrc(wobase, DH * DM * 2);
  const __amdgpu_buffer_rsrc_t rx = mk_rsrc(B.x, DM * 2);
  const __amdgpu_buffer_rsrc_t rws = mk_rsrc(B.ws, WS_BYTES);

  if (PROBE == 1) { asm volatile("" :: "s"(B.k), "s"(B.x), "s"(B.ws)); }
  PROBE_AT(1);
  unsigned xv[5];
  v4u wq[5], ring[16], kb[64];
  v2u wm[32];
  unsigned we[16];
  const int lo = lane * 16;
  const int kv_s = (w & 1) * KB_i + ((w >> 1) & 1) * KB_j + (w >> 2) * KB_Q + (sp & 1) * KB_p + (sp >> 1) * KB_P;
  const int row0 = sp * 160 + w * 4;
  const int wq_v = g * 8192 + c * 16, wq_s = row0 * 8192 + h * 256;
  const int wm_v = g * (32 * 1280) + c * 8, we_v = g * (32 * 1280) + (c >> 3) * 1280 + (c & 7) * 4;
#define WO_ROW(kk) ((4 * (((kk) ^ sp) & 1) + (((kk) ^ sp) & 2)) * 1280)
#define LD_O(n) do { constexpr int kk_ = (n) / 12, u_ = (n) % 12; \
    if (u_ < 8) wm[kk_ * 8 + (u_ & 7)] = __builtin_amdgcn_raw_buffer_load_b64(rwo, wm_v + (u_ & 1) * 1280, WO_ROW(kk_) + w * 128 + (u_ >> 1) * (8 * 1280), WO_AUX); \
    else we[kk_ * 4 + (u_ & 3)] = __builtin_amdgcn_raw_buffer_load_b32(rwo, we_v + 1024, WO_ROW(kk_) + w * 32 + (u_ & 3) * (8 * 1280), WO_AUX); } while (0)
#define LD_KV(e, rs, so) ring[e] = __builtin_amdgcn_raw_buffer_load_b128(rs, lo + ((e) & 1) * KB_a + (((e) >> 1) & 1) * KB_b, (so) + (((e) >> 2) & 1) * KB_c + ((e) >> 3) * KB_d, KV_AUX)
  const __amdgpu_buffer_rsrc_t rv = mk_rsrc(vbase, HEAD_BYTES);
#define LD_N(n) do { if ((n) < 64) kb[(n) & 63] = __builtin_amdgcn_raw_buffer_load_b128(rk, lo + ((n) & 1) * KB_a + (((n) >> 1) & 1) * KB_b, kv_s + KV_STEP((n) >> 4) + (((n) >> 2) & 1) * KB_c + (((n) >> 3) & 1) * KB_d, KV_AUX); \
    else LD_KV((n) & 15, rv, kv_s); } while (0)
#define ISSUE_SYNC() do { SCHED_FENCE(); __builtin_amdgcn_s_barrier(); SCHED_FENCE(); } while (0)
#pragma unroll
  for (int u = 0; u < 5; ++u) wq[u] = __builtin_amdgcn_raw_buffer_load_b128(rwq, wq_v, wq_s + u * (32 * 8192), WQ_AUX);
#pragma unroll
  for (int u = 0; u < 5; ++u) xv[u] = __builtin_amdgcn_raw_buffer_load_b16(rx, g * 2 + u * 64, row0 * 2, 0);
  int eoff = WS_EPOCH + blk * 256;
  asm volatile("" : "+v"(eoff));
  const v2u ep = __builtin_amdgcn_raw_buffer_load_b64(rws, eoff, 0, AUX_SC1);
  SCHED_FENCE();
  const int red = sp < 2 && !(ABL & 2) ? h + 32 * sp : -1;
  const bool keeper = sp == 3 && h == KEEP_H;
  const int o_y = WS_SLAB + blk * SLAB_BLK + red * (128 * 128) + tid * 32, o_ml = WS_ML + (blk * 128 + (tid >> 2)) * 128 + (red & 7) * 16;
  const unsigned tQ = __builtin_amdgcn_raw_buffer_load_b32(rws, tid < (QTOUCH & 1 ? 32 : 8) ? WS_Q + hg * 4096 + (QTOUCH & 1 ? 0 : sp * 1024) + tid * 128 : OOB, 0, QTOUCH > 2 ? 0 : AUX_SC1);
  SCHED_FENCE();
  if (PROLOG & 1) ISSUE_SYNC();
#pragma unroll
  for (int e = 0; e < K_EARLY; ++e) { if (e < K_EARLY0 || w != 0) LD_N(e); SCHED_FENCE(); }
  SCHED_FENCE();
  asm volatile("" :: "v"(cpf));
  asm volatile("" :: "v"(tQ));
  v4u sel;
  {
    const unsigned src = c < 8 ? 0x0504u : 0x0706u;
    const unsigned on = (c & 1) ? ((src << 16) | 0x0c0cu) : (0x0c0c0000u | src);
    const int jj = (c & 7) >> 1;
#pragma unroll
    for (int j = 0; j < 4; ++j) sel[j] = jj == j ? on : 0x0c0c0c0cu;
  }
#define AFRAG(pk) v4u{__builtin_amdgcn_perm(pk, 0u, sel[0]), __builtin_amdgcn_perm(pk, 0u, sel[1]), __builtin_amdgcn_perm(pk, 0u, sel[2]), __builtin_amdgcn_perm(pk, 0u, sel[3])}
  {
    f32x4 d = {0.f, 0.f, 0.f, 0.f};
#pragma unroll
    for (int u = 0; u < 5; ++u) d = mfma16(AFRAG(xv[u]), wq[u], d);
    if (g < 2) *(f32x4*)&lds[L_Q + w * 128 + 8 * c + 4 * g] = d;
    if (tid == 0) lds[L_CNT] = 0.f;
  }
  const u64 e64 = ((u64)ep[1] << 32) | ep[0];
  const u64 t64 = e64 * 6364136223846793005ull + 1442695040888963407ull;
  unsigned tagA = (unsigned)t64, tagB = (unsigned)(t64 >> 32);
  asm volatile("" : "+v"(tagA), "+v"(tagB));
  WG_BARRIER();
  SCHED_FENCE();
  if (PROBE == 6) STAMP(1);
  if (PROBE == 11) STAMP(4);
  const int qb = WS_Q + hg * 4096;
  f32x2 qs = {0.f, 0.f};
  if (w == 0) {
#pragma unroll
    for (int ww = 0; ww < NW; ++ww) qs += *(const f32x2*)&lds[L_Q + ww * 128 + 2 * lane];
    __builtin_amdgcn_raw_buffer_store_b128(v4u{__float_as_uint(qs[0]), tagA, __float_as_uint(qs[1]), tagB}, rws, lo, qb + sp * 1024, AUX_SC1);
  }
  SCHED_FENCE();
  const __amdgpu_buffer_rsrc_t rp = mk_rsrc(B.ws, w == 0 ? WS_BYTES : 0);
  v4u pa[3][3];
#define Q_POLL(n, rs) do { _Pragma("unroll") for (int i_ = 0; i_ < 3; ++i_) pa[n][i_] = __builtin_amdgcn_raw_buffer_load_b128(rs, lo, qb + ((sp + 1 + i_) & 3) * 1024, AUX_SC1); SCHED_FENCE(); } while (0)
#define Q_OK(n) (__builtin_amdgcn_ballot_w64(((pa[n][0][1] ^ tagA) | (pa[n][0][3] ^ tagB) | (pa[n][1][1] ^ tagA) | (pa[n][1][3] ^ tagB) | (pa[n][2][1] ^ tagA) | (pa[n][2][3] ^ tagB)) != 0) == 0)
#define Q_AT(pos) do { if (QP0 == (pos)) Q_POLL(0, rp); if (QP1 == (pos)) Q_POLL(1, rp); if (QP2 == (pos)) Q_POLL(2, rp); } while (0)
#pragma unroll
  for (int e = K_EARLY; e < 16; ++e) { Q_AT(e); LD_N(e); SCHED_FENCE(); }
  Q_AT(16);
  LD_O(0); LD_O(1); LD_O(2); LD_O(3); SCHED_FENCE(); Q_AT(17); LD_O(4); LD_O(5); LD_O(6); LD_O(7); SCHED_FENCE(); Q_AT(18); LD_O(8); LD_O(9); LD_O(10); LD_O(11);
  SCHED_FENCE();
  Q_AT(19);
  if (PROBE == 0 || PROBE == 10) STAMP(1);
  if (w == 0) {
    if (PROBE == 11) STAMP(5);
    bool ok = Q_OK(0);
    if (QP1 < 99 && !ok) { ok = Q_OK(1); pa[0][0] = pa[1][0]; pa[0][1] = pa[1][1]; pa[0][2] = pa[1][2]; }
    if (QP2 < 99 && !ok) { ok = Q_OK(2); pa[0][0] = pa[2][0]; pa[0][1] = pa[2][1]; pa[0][2] = pa[2][2]; }
#pragma nounroll
    for (int n = 0; n < POLL_MAX && !ok; ++n) {
      asm volatile("" ::: "memory");
      Q_POLL(0, rws);
      ok = Q_OK(0);
    }
    const float q0 = (qs[0] + __uint_as_float(pa[0][1][0])) + (__uint_as_float(pa[0][0][0]) + __uint_as_float(pa[0][2][0]));
    const float q1 = (qs[1] + __uint_as_float(pa[0][1][2])) + (__uint_as_float(pa[0][0][2]) + __uint_as_float(pa[0][2][2]));
    lds[L_QB + lane] = __uint_as_float(pack_bf16(q0, q1));
    if (PROBE == 11) STAMP(6);
  }
  WG_BARRIER();
  const v4u qp = *(const v4u*)&lds[L_QB + 4 * c];
  SCHED_FENCE();
  STAMP(2);

  float sc0 = 0.f, sc1 = 0.f, sc2 = 0.f, sc3;
#define TR_STEP(HALF, CTRL) _Pragma("unroll") for (int i = 0; i < (HALF); ++i) { \
      const bool up = (c & (HALF)) != 0; const float keep = up ? x[i + (HALF)] : x[i], send = up ? x[i] : x[i + (HALF)]; \
      x[i] = keep + dpp<CTRL>(send); }
#define K_EL(e, S) { constexpr int i_ = 16 * (S) + (e); \
      float d = dot2(kb[i_][0], qp[0], 0.f); \
      d = dot2(kb[i_][1], qp[1], d); d = dot2(kb[i_][2], qp[2], d); d = dot2(kb[i_][3], qp[3], d); \
      asm volatile("" : "+v"(d)); \
      x[e] = d; \
      SCHED_FENCE(); \
      if ((S) < 3 && ((e) & 3) != 3 && !(ABL & 64)) LD_O((((S) + 1) & 3) * 12 + ((e) >> 2) * 3 + ((e) & 3) % 3); \
      if (issued(i_ + 1) > issued(i_)) LD_N(issued(i_)); \
      if (issued(i_ + 1) > issued(i_) + 1) LD_N(issued(i_) + 1); \
      SCHED_FENCE(); }
#define K_STEP(S) { float x[16]; \
    K_EL(0, S) K_EL(1, S) K_EL(2, S) K_EL(3, S) K_EL(4, S) K_EL(5, S) K_EL(6, S) K_EL(7, S) \
    K_EL(8, S) K_EL(9, S) K_EL(10, S) K_EL(11, S) K_EL(12, S) K_EL(13, S) K_EL(14, S) K_EL(15, S) \
    TR_STEP(8, 0x128) TR_STEP(4, 0x141) TR_STEP(2, 0x4E) TR_STEP(1, 0xB1) \
    sc3 = x[0]; asm volatile("" : "+v"(sc3)); SCHED_FENCE(); }
  K_STEP(0)
  sc0 = sc3;
  K_STEP(1)
  if (QP1 < 99) asm volatile("" :: "v"(pa[1][0]), "v"(pa[1][1]), "v"(pa[1][2]));
  if (QP2 < 99) asm volatile("" :: "v"(pa[2][0]), "v"(pa[2][1]), "v"(pa[2][2]));
  sc1 = sc3;
  if (PROBE == 10) STAMP(4);
  K_STEP(2)
  sc2 = sc3;
  K_STEP(3)
  if (PROBE == 10) STAMP(5);
  v4u wo[20];
  if (ABL & 64) { for (int i = 8; i < 32; ++i) wm[i] = wm[i & 7] + i; for (int i = 4; i < 16; ++i) we[i] = we[i & 3] + i; }
  {
    const unsigned es = c < 8 ? 0x01000504u : 0x07060302u;
#pragma unroll
    for (int kk = 0; kk < 4; ++kk) {
#pragma unroll
      for (int i = 0; i < 4; ++i)
#pragma unroll
        for (int j = 0; j < 4; ++j) wo[kk * 5 + i][j] = __builtin_amdgcn_perm(wm[kk * 8 + 2 * j + 1][i >> 1], wm[kk * 8 + 2 * j][i >> 1], (i & 1) ? 0x07060302u : 0x05040100u);
#pragma unroll
      for (int j = 0; j < 4; ++j) wo[kk * 5 + 4][j] = __builtin_amdgcn_perm(we[kk * 4 + j], (unsigned)__builtin_amdgcn_mov_dpp((int)we[kk * 4 + j], 0x128, 0xf, 0xf, true), es);
#pragma unroll
      for (int i = 0; i < 5; ++i) asm volatile("" : "+v"(wo[kk * 5 + i]));
      SCHED_FENCE();
    }
  }
  float m, l;
  unsigned pk0, pk1, pk2;
  float p3;
  {
    const unsigned r01 = pack_bf16(sc0, sc1), r23 = pack_bf16(sc2, sc3);
    const unsigned s01 = pack_bf16(bf_lo(r01) * SCALE, bf_hi(r01) * SCALE), s23 = pack_bf16(bf_lo(r23) * SCALE, bf_hi(r23) * SCALE);
    const float s0 = bf_lo(s01), s1 = bf_hi(s01), s2 = bf_lo(s23), s3 = bf_hi(s23);
    m = xrow_max(row_max16(fmaxf(fmaxf(s0, s1), fmaxf(s2, s3))));
    const float p0 = __builtin_amdgcn_exp2f((s0 - m) * LOG2E), p1 = __builtin_amdgcn_exp2f((s1 - m) * LOG2E);
    const float p2 = __builtin_amdgcn_exp2f((s2 - m) * LOG2E);
    p3 = __builtin_amdgcn_exp2f((s3 - m) * LOG2E);
    l = xrow_sum(row_sum16((p0 + p1) + (p2 + p3)));
    pk0 = split_bf16(p0); pk1 = split_bf16(p1); pk2 = split_bf16(p2);
  }
  f32x4 acc = {0.f, 0.f, 0.f, 0.f};
#define PIN(x) asm volatile("" : "+v"(x))
  int a_aw = (g ^ sp) * 1024 + (c >> 2) * 256 + ((w + (c >> 2) + 4 * (g & 1)) & 7) * 16 + (c & 3) * 4;
  int a_st = (8 * w + (c >> 1)) * (128 * 128) + ((c & 1) * 4 + g) * 16;
  const int cc = 2 * (red & 7) + (c >> 3);
  const int col = (c & 1) ? 512 + 16 * (red >> 3) + 2 * (cc & 7) + (cc >> 3) : 64 * (red >> 3) + 4 * cc + (c >> 2 & 1) + (c & 2);
  int a_o = lane < 16 && ((c & 1) == 0 || (c & 7) == 1) ? 2 * (blk * DM + col) : OOB;
  int a_rw = c < 4 ? 4 * ((4 * c + 2 * (g & 1) + (g >> 1)) * NW + w) : 4 * (16 * NW + lane), a_rr = 4 * c * NW;
  int a_y = red >= 0 ? o_y : OOB, a_ml = red >= 0 || keeper ? o_ml : OOB, s_st = WS_SLAB + blk * SLAB_BLK + (h * 4 + sp) * 128;
  PIN(a_aw); PIN(a_st); PIN(a_o); PIN(a_rw); PIN(a_rr); PIN(a_y); PIN(a_ml);
  int a_cn = lane == 0 ? 0 : 4 * (L_R - L_CNT + 16 * NW + lane);
  PIN(a_cn);
  asm volatile("" : "+s"(s_st));
  int nth = 0;
  if (lane == 0) {
    *(f32x2*)&lds[L_ML + 2 * w] = f32x2{m, l};
    nth = __hip_atomic_fetch_add((int*)&lds[L_CNT], 1, __ATOMIC_ACQ_REL, __HIP_MEMORY_SCOPE_WORKGROUP);
  }
  if (__builtin_amdgcn_readfirstlane(nth) == NW - 1) {
    const f32x2 ml = *(const f32x2*)&lds[L_ML + 2 * (c & 7)];
    float M = fmaxf(ml[0], dpp<0xB1>(ml[0]));
    M = fmaxf(M, dpp<0x4E>(M)); M = fmaxf(M, dpp<0x141>(M));
    float L = __builtin_amdgcn_exp2f((ml[0] - M) * LOG2E) * ml[1];
    L += dpp<0xB1>(L); L += dpp<0x4E>(L); L += dpp<0x141>(L);
    if (lane < 8)
      __builtin_amdgcn_raw_buffer_store_b128(v4u{__float_as_uint(M), tagA, __float_as_uint(L), tagB}, rws, lo, WS_ML + (blk * 128 + h * 4 + sp) * 128, AUX_SC1);
  }
#define LDS_AT(T, base, off) (*(T*)((char*)&lds[base] + (off)))
#define PV_STEP(e, LOAD) { const unsigned pb = (unsigned)__builtin_amdgcn_mov_dpp((int)pk0, 0x150 + (e), 0xf, 0xf, true); \
      acc = mfma16(AFRAG(pb), ring[e], acc); SCHED_FENCE(); \
      if (LOAD) { LD_KV(e, rv, sn); SCHED_FENCE(); } }
#define PV_16(LOAD) PV_STEP(0, LOAD) PV_STEP(1, LOAD) PV_STEP(2, LOAD) PV_STEP(3, LOAD) PV_STEP(4, LOAD) PV_STEP(5, LOAD) PV_STEP(6, LOAD) PV_STEP(7, LOAD) \
    PV_STEP(8, LOAD) PV_STEP(9, LOAD) PV_STEP(10, LOAD) PV_STEP(11, LOAD) PV_STEP(12, LOAD) PV_STEP(13, LOAD) PV_STEP(14, LOAD) PV_STEP(15, LOAD)
#pragma unroll 1
  for (int it = 0; it < NSTEP - 1; ++it) {
    const int sn = kv_s + KV_STEP(it + 1);
    if (STEP_SYNC & 2) ISSUE_SYNC();
    PV_16(true)
    pk0 = pk1; pk1 = pk2;
  }
  v4u u0, u1, u2;
#define PREPOLL() do { u2 = __builtin_amdgcn_raw_buffer_load_b128(rws, a_ml, 0, AUX_SC1); \
    u0 = __builtin_amdgcn_raw_buffer_load_b128(rws, a_y, 0, P_AUX); \
    u1 = __builtin_amdgcn_raw_buffer_load_b128(rws, a_y, 16, P_AUX); SCHED_FENCE(); } while (0)
  if (PROBE == 10) STAMP(6);
  if (PRE == 1) PREPOLL();
  WG_BARRIER();
  if (PRE == 2) PREPOLL();
  {
    const f32x2 ml = *(const f32x2*)&lds[L_ML + 2 * (c & 7)];
    float M = fmaxf(ml[0], dpp<0xB1>(ml[0]));
    M = fmaxf(M, dpp<0x4E>(M)); M = fmaxf(M, dpp<0x141>(M));
    const float f = __builtin_amdgcn_exp2f((m - M) * LOG2E);
    pk0 = split_bf16(p3 * f);
    acc *= f;
    SCHED_FENCE();
    const int sn = 0;
    PV_16(false)
  }
  if (PROBE == 5) STAMP(1);
  {
    const float a0 = fold32(acc[0], acc[2]), a1 = fold32(acc[1], acc[3]);
    const unsigned hi = pack_bf16(a0, a1);
    LDS_AT(unsigned, L_A, a_aw) = hi;
    LDS_AT(unsigned, L_A + 32, a_aw) = pack_bf16(a0 - bf_lo(hi), a1 - bf_hi(hi));
  }
  WG_BARRIER();
  STAMP(3);
  {
    v4u af[4];
#pragma unroll
    for (int kk = 0; kk < 4; ++kk) af[kk] = LDS_AT(v4u, L_A + 256 * kk, lo);
    SCHED_FENCE();
    f32x4 y[5];
    float s[5];
#pragma unroll
    for (int i = 0; i < 5; ++i) y[i] = mfma16(af[0], wo[i], f32x4{0.f, 0.f, 0.f, 0.f});
    SCHED_FENCE();
#pragma unroll
    for (int i = 0; i < 5; ++i) y[i] = mfma16(af[1], wo[5 + i], y[i]);
#define Y_FIN(i) SCHED_FENCE(); y[i] = mfma16(af[2], wo[10 + (i)], y[i]); y[i] = mfma16(af[3], wo[15 + (i)], y[i]); SCHED_FENCE();
#define Y_SUM(i) s[i] = (y[i][0] + y[i][2]) + (y[i][1] + y[i][3]);
    Y_FIN(0) Y_FIN(1) Y_SUM(0) Y_FIN(2) Y_SUM(1)
    const float s01 = fold32(s[0], s[1]);
    Y_FIN(3) Y_SUM(2) Y_FIN(4) Y_SUM(3)
    const float tt = fold16(s01, fold32(s[2], s[3]));
    SCHED_FENCE();
    Y_SUM(4)
    if (PROBE < 10) STAMP(4);
    __builtin_amdgcn_raw_buffer_store_b128(v4u{__float_as_uint(tt), tagA, __float_as_uint(s[4]), tagB}, rws, a_st, s_st, Y_AUX);
  }

  if (PROBE < 10) STAMP(5);
  if (red < 0 && !keeper) { STAMP_FLUSH(); return; }
  {
    int n = 0;
    if (PRE == 0) PREPOLL();
#pragma nounroll
    for (; n < POLL_MAX && (u2[1] != tagA || u2[3] != tagB); ++n) {
      asm volatile("" ::: "memory");
      u2 = __builtin_amdgcn_raw_buffer_load_b128(rws, a_ml, 0, AUX_SC1);
    }
    if (keeper) {
      WG_BARRIER();
      if (tid < 8 && !(ABL & 8)) __builtin_amdgcn_raw_buffer_store_b128(v4u{tagA, tagB, tagA, tagB}, rws, lo, WS_EPOCH + blk * 256, AUX_SC1);
      STAMP_FLUSH();
      return;
    }
    float cf;
    {
      const float mp = __uint_as_float(u2[0]), lp = __uint_as_float(u2[2]);
      float M = fmaxf(mp, dpp<0x128>(mp)); M = fmaxf(M, dpp<0x124>(M));
      const float f = __builtin_amdgcn_exp2f((mp - M) * LOG2E);
      float L = f * lp; L += dpp<0x128>(L); L += dpp<0x124>(L);
      cf = f * __builtin_amdgcn_rcpf(L);
      PIN(cf);
    }
#define Y_MISS (((u0[1] ^ tagA) | (u0[3] ^ tagB) | (u1[1] ^ tagA) | (u1[3] ^ tagB)) != 0)
    n = 0;
    if (Y_MISS)
#pragma nounroll
    do {
      asm volatile("" ::: "memory");
      u0 = __builtin_amdgcn_raw_buffer_load_b128(rws, a_y, 0, P_AUX);
      u1 = __builtin_amdgcn_raw_buffer_load_b128(rws, a_y, 16, P_AUX);
    } while (++n < ((ABL & 32) ? POLL : POLL_MAX) && Y_MISS);
    if (PROBE < 10) STAMP(6);
    f32x4 v = {__uint_as_float(u0[0]) * cf, __uint_as_float(u0[2]) * cf,
               __uint_as_float(u1[0]) * cf, __uint_as_float(u1[2]) * cf};
    float t = fold16(fold32(v[0], v[1]), fold32(v[2], v[3]));
    t += dpp<0x128>(t); t += dpp<0x124>(t);
    LDS_AT(float, L_R, a_rw) = t;
    asm volatile("" ::: "memory");
    const int nd = __hip_atomic_fetch_add(&LDS_AT(int, L_CNT, a_cn), 1, __ATOMIC_RELAXED, __HIP_MEMORY_SCOPE_WORKGROUP);
    asm volatile("" ::: "memory");
    f32x4 s4 = LDS_AT(f32x4, L_R, a_rr) + LDS_AT(f32x4, L_R + 4, a_rr);
    PIN(s4);
    if (__builtin_amdgcn_readfirstlane(nd) == 2 * NW - 1) {
      asm volatile(".Lattn_pf_to:");
      const float o = (s4[0] + s4[2]) + (s4[1] + s4[3]);
      float e = o + dpp<0x4E>(o);
      e += dpp<0x104>(e);
      __builtin_amdgcn_raw_buffer_store_b16((unsigned short)pack_bf16((c & 1) ? e : o, 0.f), mk_rsrc(B.out, (ABL & 16) ? 0 : DM * 4), a_o, 0, OUT_AUX);
    }
    STAMP(7);
    STAMP_FLUSH();
  }
}

__global__ void __launch_bounds__(NTHR, 2) attn_decode_fused(Params P) {
  __shared__ __attribute__((aligned(16))) float lds[L_END];
  const int b = blockIdx.x;
  const int hg = (b & 7) * 8 + (b >> 5);
  const int blk = MAP ? hg & 1 : hg >> 5;
  if (AUX_A == AUX_B || blk == 0) attn_body<AUX_A>(P, lds); else attn_body<AUX_B>(P, lds);
}

extern "C" void kernel_launch(void* const* d_in, const int* in_sizes, int n_in,
                              void* d_out, int out_size, void* d_ws, size_t ws_size,
                              hipStream_t stream) {
  Params P;
  for (int i = 0; i < 2; ++i) {
    P.b[i].x = d_in[5 * i]; P.b[i].k = d_in[5 * i + 1]; P.b[i].v = d_in[5 * i + 2];
    P.b[i].wq = d_in[5 * i + 3]; P.b[i].wo = d_in[5 * i + 4];
    P.b[i].out = d_out; P.b[i].ws = d_ws; P.b[i].ws2 = d_ws;
  }
  hipLaunchKernelGGL(attn_decode_fused, dim3(256), dim3(NTHR), 0, stream, P);
}
```

```cpp
#include <hip/hip_runtime.h>
#include <hip/hip_bf16.h>
#include <stdint.h>

typedef unsigned v4u __attribute__((ext_vector_type(4)));
typedef unsigned v2u __attribute__((ext_vector_type(2)));
typedef float f32x4 __attribute__((ext_vector_type(4)));
typedef float f32x2 __attribute__((ext_vector_type(2)));
typedef __bf16 bf16x2_t __attribute__((ext_vector_type(2)));
typedef __bf16 bf16x8_t __attribute__((ext_vector_type(8)));
typedef unsigned s16u __attribute__((ext_vector_type(16)));
typedef unsigned long long u64;

constexpr int DM = 640, NH = 32, SK = 8192, DH = 128;
constexpr int NW = 8, NTHR = NW * 64;
constexpr int KV_a = 1 << 10, KV_i = 1 << 11, KV_j = 1 << 12, KV_b = 1 << 13, KV_p = 1 << 14, KV_P = 1 << 15,
              KV_c = 1 << 16, KV_d = 1 << 17, KV_s = 1 << 18, KV_Q = 1 << 19, KV_S = 1 << 20;
constexpr int HEAD_BYTES = SK * DH * 2;
constexpr int NRED = 40;
constexpr int WS_EPOCH = 0;
constexpr int WS_Q = 4096;
constexpr int WS_ML = WS_Q + 256 * 1024;
constexpr int WS_SLAB = WS_ML + 256 * 128;
constexpr int SLAB_BLK = NRED * 128 * 128;
constexpr int WS_REP = WS_SLAB + 2 * SLAB_BLK;
constexpr int WS_BYTES = WS_REP + 4096;
constexpr int OOB = 0x7ffffff0;
constexpr int AUX_SC1 = 16, AUX_NT = 2;
constexpr int POLL_MAX = 1 << 21;
constexpr float SCALE = 0.08838834764831845f, LOG2E = 1.4426950408889634f;

constexpr int PG = 4;
constexpr int P0 = 0;
constexpr int P1 = 112;
constexpr int TEA = 2280;
constexpr int TSB = 2130;
constexpr int TEB = 3765;
constexpr int REARLY = 30;
constexpr int TP0 = 2800;
constexpr int TRD = 200;
constexpr int TAUG = 148;
constexpr int D0 = 3155;
constexpr int LAM = 70;
constexpr int SD0 = 70;
constexpr int MU = 50;
constexpr int G0 = 140;
constexpr int G1 = 200;
constexpr int HIC = 240;
constexpr int CM = -22;
constexpr int TLO = 40;
constexpr int TPOLL = 160;
constexpr int CW = 1;
constexpr int TRY_AT = 98;
constexpr int K_EARLY = 4;
constexpr int BL = 10;
constexpr int ADDR_AT = 64;

constexpr int L_A = 0;
constexpr int L_Q = L_A + 1024;
constexpr int L_QB = L_Q + NW * 128;
constexpr int L_ML = L_QB + 64;
constexpr int L_CNT = L_ML + 2 * NW;
constexpr int L_R = L_CNT + 4;
constexpr int L_END = L_R + 16 * NW + 64;

struct Blk {
  const void* x;
  const void* wq;
  const void* k;
  const void* v;
  const void* wo;
  void* out;
  void* ws;
  void* ws2;
};
struct Params { Blk b[2]; };

__device__ __forceinline__ float dot2(unsigned a, unsigned b, float c) {
  return __builtin_amdgcn_fdot2_f32_bf16(__builtin_bit_cast(bf16x2_t, a), __builtin_bit_cast(bf16x2_t, b), c, false);
}
template <int CTRL> __device__ __forceinline__ float dpp(float x) {
  return __builtin_bit_cast(float, __builtin_amdgcn_mov_dpp(__builtin_bit_cast(int, x), CTRL, 0xf, 0xf, true));
}
__device__ __forceinline__ float row_sum16(float x) {
  x += dpp<0x128>(x); x += dpp<0x124>(x); x += dpp<0x122>(x); x += dpp<0x121>(x); return x;
}
__device__ __forceinline__ float row_max16(float x) {
  x = fmaxf(x, dpp<0x128>(x)); x = fmaxf(x, dpp<0x124>(x)); x = fmaxf(x, dpp<0x122>(x)); x = fmaxf(x, dpp<0x121>(x)); return x;
}
__device__ __forceinline__ unsigned pack_bf16(float lo, float hi) {
  bf16x2_t r = {(__bf16)lo, (__bf16)hi};
  return __builtin_bit_cast(unsigned, r);
}
__device__ __forceinline__ float bf_lo(unsigned u) { return __builtin_bit_cast(float, u << 16); }
__device__ __forceinline__ float bf_hi(unsigned u) { return __builtin_bit_cast(float, u & 0xffff0000u); }
__device__ __forceinline__ unsigned split_bf16(float a) {
  const float hi = bf_lo(pack_bf16(a, a));
  return pack_bf16(hi, a - hi);
}
__device__ __forceinline__ float half_sum(float x) {
  const auto t = __builtin_amdgcn_permlane32_swap(__float_as_uint(x), __float_as_uint(x), false, false);
  return __uint_as_float(t[0]) + __uint_as_float(t[1]);
}
__device__ __forceinline__ float fold32(float a, float b) {
  const auto t = __builtin_amdgcn_permlane32_swap(__float_as_uint(a), __float_as_uint(b), false, false);
  return __uint_as_float(t[0]) + __uint_as_float(t[1]);
}
__device__ __forceinline__ float fold16(float a, float b) {
  const auto t = __builtin_amdgcn_permlane16_swap(__float_as_uint(a), __float_as_uint(b), false, false);
  return __uint_as_float(t[0]) + __uint_as_float(t[1]);
}
__device__ __forceinline__ float xrow_sum(float x) {
  const auto s = __builtin_amdgcn_permlane16_swap(__float_as_uint(x), __float_as_uint(x), false, false);
  return half_sum(__uint_as_float(s[0]) + __uint_as_float(s[1]));
}
__device__ __forceinline__ float xrow_max(float x) {
  const auto s = __builtin_amdgcn_permlane16_swap(__float_as_uint(x), __float_as_uint(x), false, false);
  x = fmaxf(__uint_as_float(s[0]), __uint_as_float(s[1]));
  const auto t = __builtin_amdgcn_permlane32_swap(__float_as_uint(x), __float_as_uint(x), false, false);
  return fmaxf(__uint_as_float(t[0]), __uint_as_float(t[1]));
}
__device__ __forceinline__ f32x4 mfma16(v4u a, v4u b, f32x4 c) {
  return __builtin_amdgcn_mfma_f32_16x16x32_bf16(__builtin_bit_cast(bf16x8_t, a), __builtin_bit_cast(bf16x8_t, b), c, 0, 0, 0);
}
__device__ __forceinline__ __amdgpu_buffer_rsrc_t mk_rsrc(const void* p, int bytes) {
  return __builtin_amdgcn_make_buffer_rsrc((void*)p, 0, bytes, 0x00020000);
}
#define SCHED_FENCE() __builtin_amdgcn_sched_barrier(0)
#define WG_BARRIER() do { asm volatile("s_waitcnt lgkmcnt(0)" ::: "memory"); __builtin_amdgcn_s_barrier(); asm volatile("" ::: "memory"); } while (0)
#define PIN(x) asm volatile("" : "+v"(x))

__global__ void __launch_bounds__(NTHR, 2) attn_decode_fused(Params P) {
  __shared__ __attribute__((aligned(16))) float smem[L_END];
  float* const lds = smem;
  const int b = blockIdx.x, tid = threadIdx.x;
  s16u ka;
  u64 t0_;
  asm volatile("s_memrealtime %1\n\ts_load_dwordx16 %0, %2, %3" : "=&s"(ka), "=&s"(t0_) : "s"(__builtin_amdgcn_kernarg_segment_ptr()), "s"((b & 4) << 4));
  unsigned cpf;
  {
    u64 pc; int len;
    asm volatile("s_getpc_b64 %0\n.Lattn_pf_from:\n\ts_mov_b32 %1, .Lattn_pf_to - .Lattn_pf_from" : "=s"(pc), "=s"(len));
    cpf = __builtin_amdgcn_raw_buffer_load_b32(mk_rsrc((const void*)pc, len < 0 ? 0 : len), tid * 128, 0, 0);
    SCHED_FENCE();
  }
  const int hg = (b & 7) * 8 + (b >> 5);
  const int sp = (b >> 3) & 3;
  const int blk = hg >> 5, h = hg & 31;
  const int lane = tid & 63, w = __builtin_amdgcn_readfirstlane(tid >> 6);
  const int g = lane >> 4, c = lane & 15;

  int wq_s = (sp * 160 + w * 4) * 8192 + h * 256, wq_v = g * 8192 + c * 16, lo = lane * 16, q_v = tid < 8 ? WS_Q + hg * 4096 + sp * 1024 + tid * 128 : OOB;
  int kv_s = (w & 1) * KV_i + ((w >> 1) & 1) * KV_j + (w >> 2) * KV_Q + (sp & 1) * KV_p + (sp >> 1) * KV_P;
  kv_s += ((hg ^ (hg >> 3)) & 7) * KV_c + (((hg >> 3) ^ (hg >> 4)) & 1) * KV_S;
  asm volatile("s_waitcnt lgkmcnt(0)" : "+s"(ka), "+s"(t0_), "+s"(wq_s), "+s"(kv_s), "+v"(wq_v), "+v"(lo), "+v"(q_v));
  const Blk B = __builtin_bit_cast(Blk, ka);
  const unsigned t_in = (unsigned)t0_;
  const size_t kv_off = (size_t)h * HEAD_BYTES;
  const __amdgpu_buffer_rsrc_t rk = mk_rsrc((const char*)B.k + kv_off, HEAD_BYTES);
  const __amdgpu_buffer_rsrc_t rv = mk_rsrc((const char*)B.v + kv_off, HEAD_BYTES);
  const __amdgpu_buffer_rsrc_t rwo = mk_rsrc((const char*)B.wo + (size_t)h * DH * DM * 2, DH * DM * 2);
  const __amdgpu_buffer_rsrc_t rwq = mk_rsrc(B.wq, DM * NH * DH * 2);
  const __amdgpu_buffer_rsrc_t rx = mk_rsrc(B.x, DM * 2);
  const __amdgpu_buffer_rsrc_t rws = mk_rsrc(B.ws, WS_BYTES);

  unsigned xv[5];
  v4u wq[5], kb[128];
  v2u wm[32];
  unsigned we[16];
  const int row0 = sp * 160 + w * 4;
  const int wm_v = g * (32 * 1280) + c * 8, we_v = g * (32 * 1280) + (c >> 3) * 1280 + (c & 7) * 4;
#define WO_ROW(kk) ((4 * (((kk) ^ sp) & 1) + (((kk) ^ sp) & 2)) * 1280)
#define LD_O(n) do { constexpr int kk_ = (n) / 12, u_ = (n) % 12; \
    if (u_ < 8) wm[kk_ * 8 + (u_ & 7)] = __builtin_amdgcn_raw_buffer_load_b64(rwo, wm_v + (u_ & 1) * 1280, WO_ROW(kk_) + w * 128 + (u_ >> 1) * (8 * 1280), 0); \
    else we[kk_ * 4 + (u_ & 3)] = __builtin_amdgcn_raw_buffer_load_b32(rwo, we_v + 1024, WO_ROW(kk_) + w * 32 + (u_ & 3) * (8 * 1280), 0); } while (0)
#define LD_N(n) do { if ((n) < 128) kb[(n) & 127] = __builtin_amdgcn_raw_buffer_load_b128((n) < 64 ? rk : rv, lo + ((n) & 1) * KV_a + (((n) >> 1) & 1) * KV_b, \
      kv_s ^ ((((n) >> 2) & 1) * KV_c + (((n) >> 3) & 1) * KV_d + (((n) >> 4) & 1) * KV_s + (((n) >> 5) & 1) * KV_S), AUX_NT); } while (0)
  const int te = (w < 4 ? TEA - TEB : 0) - (sp < 2 ? REARLY : 0);
  int dl, mg = 0, pv;
  unsigned pt = 0;
#define PACE(i) do { if ((i) >= P0 && (i) < P1 && (i) % PG == 0) { \
      asm volatile("ds_add_rtn_u32 %[pt], %[lo], %[lo] offset:%[of]\n" \
        "1:\n\ts_memrealtime vcc\n\ts_waitcnt lgkmcnt(0)\n\ts_sub_i32 vcc_hi, vcc_lo, %[dl]\n\ts_cmp_ge_i32 vcc_hi, 0\n\ts_cbranch_scc1 2f\n\t" \
        "s_cmp_lt_i32 vcc_hi, -6000\n\ts_cbranch_scc1 2f\n\ts_sleep 1\n\ts_branch 1b\n" \
        "2:\n\tv_readfirstlane_b32 %[dl], %[pt]\n\ts_sub_i32 vcc_hi, %[te], vcc_lo\n\ts_add_i32 vcc_hi, vcc_hi, %[dl]\n\ts_mul_i32 vcc_hi, vcc_hi, %[sh]\n\t" \
        "s_ashr_i32 vcc_hi, vcc_hi, 14\n\ts_add_i32 %[dl], vcc_lo, vcc_hi\n\ts_sub_i32 vcc_hi, vcc_lo, %[pv]\n\ts_max_i32 %[mg], %[mg], vcc_hi\n\ts_mov_b32 %[pv], vcc_lo" \
        : [dl] "+s"(dl), [pt] "+v"(pt), [mg] "+s"(mg), [pv] "+s"(pv) \
        : [te] "s"(te), [sh] "n"(16384 / ((i) < P1 - PG ? (P1 - (i)) / PG - 1 : 1)), [lo] "v"(lo), [of] "n"(4 * L_Q) : "vcc", "scc", "memory"); SCHED_FENCE(); } } while (0)
#define NOW() ((int)((unsigned)__builtin_amdgcn_s_memrealtime() - t_in))
#define UNTIL(t) do { _Pragma("nounroll") for (int n_ = 0; n_ < 4096 && NOW() < (t); ++n_) __builtin_amdgcn_s_sleep(1); } while (0)
#define LDS_GET(i) __builtin_amdgcn_readfirstlane(__hip_atomic_load((int*)&lds[i], __ATOMIC_RELAXED, __HIP_MEMORY_SCOPE_WORKGROUP))
#pragma unroll
  for (int u = 0; u < 5; ++u) wq[u] = __builtin_amdgcn_raw_buffer_load_b128(rwq, wq_v, wq_s + u * (32 * 8192), AUX_NT);
#pragma unroll
  for (int u = 0; u < 5; ++u) xv[u] = __builtin_amdgcn_raw_buffer_load_b16(rx, g * 2 + u * 64, row0 * 2, 0);
  int eoff = WS_EPOCH;
  PIN(eoff);
  const v2u ep = __builtin_amdgcn_raw_buffer_load_b64(rws, eoff, 0, AUX_SC1);
  SCHED_FENCE();
  const int red = ((h >> 3) * 10 + (h & 7) * 2 + sp) | ((1 - sp) >> 31) | ((4 - (h & 7)) >> 31);
  const bool keeper = sp == 3 && hg == 2 * NH - 1;
  const unsigned tQ = __builtin_amdgcn_raw_buffer_load_b32(rws, q_v, 0, AUX_SC1);
  SCHED_FENCE();
  __builtin_amdgcn_s_barrier();
  SCHED_FENCE();
#pragma unroll
  for (int e = 0; e < K_EARLY; ++e) { LD_N(e); SCHED_FENCE(); }
  v4u sel;
  {
    const unsigned src = c < 8 ? 0x0504u : 0x0706u;
    const unsigned on = (c & 1) ? ((src << 16) | 0x0c0cu) : (0x0c0c0000u | src);
    const int jj = (c & 7) >> 1;
#pragma unroll
    for (int j = 0; j < 4; ++j) sel[j] = jj == j ? on : 0x0c0c0c0cu;
  }
  PIN(sel);
  SCHED_FENCE();
  asm volatile("" :: "v"(cpf));
  asm volatile("" :: "v"(tQ));
#define AFRAG(pk) v4u{__builtin_amdgcn_perm(pk, 0u, sel[0]), __builtin_amdgcn_perm(pk, 0u, sel[1]), __builtin_amdgcn_perm(pk, 0u, sel[2]), __builtin_amdgcn_perm(pk, 0u, sel[3])}
  {
    f32x4 d = {0.f, 0.f, 0.f, 0.f};
#pragma unroll
    for (int u = 0; u < 5; ++u) d = mfma16(AFRAG(xv[u]), wq[u], d);
    if (g < 2) *(f32x4*)&lds[L_Q + w * 128 + 8 * c + 4 * g] = d;
    if (tid == 0) lds[L_CNT] = 0.f;
  }
  const u64 e64 = ((u64)ep[1] << 32) | ep[0];
  const u64 t64 = e64 * 6364136223846793005ull + 1442695040888963407ull;
  unsigned tagA = (unsigned)t64, tagB = (unsigned)(t64 >> 32);
  PIN(tagA); PIN(tagB);
  WG_BARRIER();
  SCHED_FENCE();
  dl = (int)t_in + (w < 4 ? 0 : TSB);
  const int qb = WS_Q + hg * 4096;
  f32x2 qs = {0.f, 0.f};
  if (w == 0) {
#pragma unroll
    for (int ww = 0; ww < NW; ++ww) qs += *(const f32x2*)&lds[L_Q + ww * 128 + 2 * lane];
    __builtin_amdgcn_raw_buffer_store_b128(v4u{__float_as_uint(qs[0]), tagA, __float_as_uint(qs[1]), tagB}, rws, lo, qb + sp * 1024, AUX_SC1);
  }
  SCHED_FENCE();
  v4u pa[3];
#define Q_POLL(rs) do { _Pragma("unroll") for (int i_ = 0; i_ < 3; ++i_) pa[i_] = __builtin_amdgcn_raw_buffer_load_b128(rs, lo, qb + ((sp + 1 + i_) & 3) * 1024, AUX_SC1); SCHED_FENCE(); } while (0)
#define Q_OK() (__builtin_amdgcn_ballot_w64(((pa[0][1] ^ tagA) | (pa[0][3] ^ tagB) | (pa[1][1] ^ tagA) | (pa[1][3] ^ tagB) | (pa[2][1] ^ tagA) | (pa[2][3] ^ tagB)) != 0) == 0)
#pragma unroll
  for (int e = K_EARLY; e < BL; ++e) { LD_N(e); SCHED_FENCE(); }
  if (w < 4) {
#pragma unroll
    for (int e = BL; e < 16; ++e) { LD_N(e); SCHED_FENCE(); }
  }
  Q_POLL(mk_rsrc(B.ws, w == 0 ? WS_BYTES : 0));
  LD_O(0); LD_O(1); LD_O(2); LD_O(3); SCHED_FENCE(); LD_O(4); LD_O(5); LD_O(6); LD_O(7); SCHED_FENCE(); LD_O(8); LD_O(9); LD_O(10); LD_O(11);
  SCHED_FENCE();
  if (w == 0) {
    bool ok = Q_OK();
#pragma nounroll
    for (int n = 0; n < POLL_MAX && !ok; ++n) {
      asm volatile("" ::: "memory");
      Q_POLL(rws);
      ok = Q_OK();
    }
    const float q0 = (qs[0] + __uint_as_float(pa[1][0])) + (__uint_as_float(pa[0][0]) + __uint_as_float(pa[2][0]));
    const float q1 = (qs[1] + __uint_as_float(pa[1][2])) + (__uint_as_float(pa[0][2]) + __uint_as_float(pa[2][2]));
    lds[L_QB + lane] = __uint_as_float(pack_bf16(q0, q1));
    lds[L_Q + 4 * lane] = __uint_as_float(lane == 0 ? t_in + TEB : 0u);
  }
  WG_BARRIER();
  const unsigned t_q = (unsigned)__builtin_amdgcn_s_memrealtime();
  pv = (int)t_q + 60;
  const v4u qp = *(const v4u*)&lds[L_QB + 4 * c];
  SCHED_FENCE();
  if (w >= 4) {
#pragma unroll
    for (int e = BL; e < 16; ++e) { LD_N(e); SCHED_FENCE(); }
  }

  float sc[4];
#define TR_STEP(HALF, CTRL) _Pragma("unroll") for (int i = 0; i < (HALF); ++i) { \
      const bool up = (c & (HALF)) != 0; const float keep = up ? x[i + (HALF)] : x[i], send = up ? x[i] : x[i + (HALF)]; \
      x[i] = keep + dpp<CTRL>(send); }
#define K_EL(e, S) { constexpr int i_ = 16 * (S) + (e); \
      float d = dot2(kb[i_][0], qp[0], 0.f); \
      d = dot2(kb[i_][1], qp[1], d); d = dot2(kb[i_][2], qp[2], d); d = dot2(kb[i_][3], qp[3], d); \
      PIN(d); \
      x[e] = d; \
      SCHED_FENCE(); \
      PACE(i_); \
      if ((S) < 3 && ((e) & 3) != 3) LD_O((((S) + 1) & 3) * 12 + ((e) >> 2) * 3 + ((e) & 3) % 3); \
      LD_N(i_ + 16); \
      SCHED_FENCE(); }
#define K_STEP(S) { float x[16]; \
    K_EL(0, S) K_EL(1, S) K_EL(2, S) K_EL(3, S) K_EL(4, S) K_EL(5, S) K_EL(6, S) K_EL(7, S) \
    K_EL(8, S) K_EL(9, S) K_EL(10, S) K_EL(11, S) K_EL(12, S) K_EL(13, S) K_EL(14, S) K_EL(15, S) \
    TR_STEP(8, 0x128) TR_STEP(4, 0x141) TR_STEP(2, 0x4E) TR_STEP(1, 0xB1) \
    sc[S] = x[0]; PIN(sc[S]); SCHED_FENCE(); }
  K_STEP(0) K_STEP(1) K_STEP(2) K_STEP(3)
  v4u wo[20];
#define WO_PERM(kk) do { \
    _Pragma("unroll") for (int i = 0; i < 4; ++i) { \
      _Pragma("unroll") for (int j = 0; j < 4; ++j) wo[(kk) * 5 + i][j] = __builtin_amdgcn_perm(wm[(kk) * 8 + 2 * j + 1][i >> 1], wm[(kk) * 8 + 2 * j][i >> 1], (i & 1) ? 0x07060302u : 0x05040100u); \
      PIN(wo[(kk) * 5 + i]); if (i & 1) SCHED_FENCE(); } \
    _Pragma("unroll") for (int j = 0; j < 4; ++j) \
      wo[(kk) * 5 + 4][j] = __builtin_amdgcn_perm(we[(kk) * 4 + j], (unsigned)__builtin_amdgcn_mov_dpp((int)we[(kk) * 4 + j], 0x128, 0xf, 0xf, true), c < 8 ? 0x01000504u : 0x07060302u); \
    PIN(wo[(kk) * 5 + 4]); SCHED_FENCE(); } while (0)
  WO_PERM(0); WO_PERM(1); WO_PERM(2); WO_PERM(3);
  float m;
  unsigned pk[4];
  {
    const unsigned r01 = pack_bf16(sc[0], sc[1]), r23 = pack_bf16(sc[2], sc[3]);
    const unsigned s01 = pack_bf16(bf_lo(r01) * SCALE, bf_hi(r01) * SCALE), s23 = pack_bf16(bf_lo(r23) * SCALE, bf_hi(r23) * SCALE);
    const float s0 = bf_lo(s01), s1 = bf_hi(s01), s2 = bf_lo(s23), s3 = bf_hi(s23);
    m = xrow_max(row_max16(fmaxf(fmaxf(s0, s1), fmaxf(s2, s3))));
    const float p0 = __builtin_amdgcn_exp2f((s0 - m) * LOG2E), p1 = __builtin_amdgcn_exp2f((s1 - m) * LOG2E);
    const float p2 = __builtin_amdgcn_exp2f((s2 - m) * LOG2E), p3 = __builtin_amdgcn_exp2f((s3 - m) * LOG2E);
    *(f32x2*)&lds[L_ML + 2 * w] = f32x2{m, xrow_sum(row_sum16((p0 + p1) + (p2 + p3)))};
    pk[0] = split_bf16(p0); pk[1] = split_bf16(p1); pk[2] = split_bf16(p2); pk[3] = split_bf16(p3);
  }
#define LDS_AT(T, base, off) (*(T*)((char*)&lds[base] + (off)))
  int a_cn = lane == 0 ? 0 : 4 * (L_R - L_CNT + 16 * NW + lane);
  PIN(a_cn);
  asm volatile("" ::: "memory");
  __hip_atomic_fetch_add(&LDS_AT(int, L_CNT, a_cn), 1, __ATOMIC_RELAXED, __HIP_MEMORY_SCOPE_WORKGROUP);
  asm volatile("" ::: "memory");
  float fw = -1.f;
#define WG_MAX() do { asm volatile("" ::: "memory"); \
    const f32x2 ml = *(const f32x2*)&lds[L_ML + 2 * (c & 7)]; \
    float M = fmaxf(ml[0], dpp<0xB1>(ml[0])); \
    M = fmaxf(M, dpp<0x4E>(M)); M = fmaxf(M, dpp<0x141>(M)); \
    if (w == 0) { \
      float L = __builtin_amdgcn_exp2f((ml[0] - M) * LOG2E) * ml[1]; \
      L += dpp<0xB1>(L); L += dpp<0x4E>(L); L += dpp<0x141>(L); \
      if (lane < 8) __builtin_amdgcn_raw_buffer_store_b128(v4u{__float_as_uint(M), tagA, __float_as_uint(L), tagB}, rws, lo, WS_ML + (blk * 128 + h * 4 + sp) * 128, AUX_SC1); \
    } \
    fw = __builtin_amdgcn_exp2f((m - M) * LOG2E); } while (0)
  bool have = false;
  int a_aw, a_st, a_o, a_rw, a_rr, a_y, a_ml, s_st;
#define ADDRESSES() do { int c_ = c, g_ = g; PIN(c_); PIN(g_); const int lane_ = 16 * g_ + c_, tid_ = 64 * w + lane_; \
    a_aw = (g_ ^ sp) * 1024 + (c_ >> 2) * 256 + ((w + (c_ >> 2) + 4 * (g_ & 1)) & 7) * 16 + (c_ & 3) * 4; \
    a_st = (g_ & 1) == 0 ? (5 * w + g_ + (c_ >> 3)) * (128 * 128) + (c_ & 7) * 16 : lane_ < 24 ? (5 * w + 4) * (128 * 128) + c_ * 16 : OOB; \
    const int rw = (red * 13) >> 6, rj = red - 5 * rw; \
    a_o = lane_ < 16 ? 2 * (blk * DM + (rj < 4 ? 64 * rw + 32 * (rj & 1) + (rj >> 1) + 2 * c_ : 512 + 16 * rw + c_)) : OOB; \
    a_rw = c_ < 4 ? 4 * ((4 * c_ + 2 * (g_ & 1) + (g_ >> 1)) * NW + w) : 4 * (16 * NW + lane_); a_rr = 4 * c_ * NW; \
    a_y = WS_SLAB + blk * SLAB_BLK + red * (128 * 128) + tid_ * 32; \
    a_ml = red >= 0 ? WS_ML + (blk * 128 + (tid_ >> 2)) * 128 + (red & 7) * 16 : keeper ? WS_ML + (tid_ >> 1) * 128 : OOB; \
    s_st = __builtin_amdgcn_readfirstlane(WS_SLAB + blk * SLAB_BLK + (h * 4 + sp) * 128); \
    PIN(a_aw); PIN(a_st); PIN(a_o); PIN(a_rw); PIN(a_rr); PIN(a_y); PIN(a_ml); asm volatile("" : "+s"(s_st)); SCHED_FENCE(); } while (0)
  f32x4 acc = {0.f, 0.f, 0.f, 0.f};
  v4u u2;
#define P_FRAG(e, S) AFRAG((unsigned)__builtin_amdgcn_mov_dpp((int)pk[S], 0x150 + (e), 0xf, 0xf, true))
#define PV_EL(e, S) { constexpr int i_ = 64 + 16 * (S) + (e); \
      if (i_ == ADDR_AT) ADDRESSES(); \
      if (i_ == TRY_AT && w >= 4 && LDS_GET(L_CNT) >= NW) { WG_MAX(); have = true; } \
      if (i_ == P1) { u2 = __builtin_amdgcn_raw_buffer_load_b128(mk_rsrc(B.ws, w >= 4 ? WS_BYTES : 0), a_ml, 0, AUX_SC1); SCHED_FENCE(); } \
      const unsigned pb = (unsigned)__builtin_amdgcn_mov_dpp((int)pk[S], 0x150 + (e), 0xf, 0xf, true); \
      acc = mfma16(AFRAG(pb), kb[i_], acc); PIN(acc); SCHED_FENCE(); \
      PACE(i_); LD_N(i_ + 16); SCHED_FENCE(); }
#define PV_12(S) PV_EL(0, S) PV_EL(1, S) PV_EL(2, S) PV_EL(3, S) PV_EL(4, S) PV_EL(5, S) PV_EL(6, S) PV_EL(7, S) PV_EL(8, S) PV_EL(9, S) PV_EL(10, S) PV_EL(11, S)
#define PV_STEP(S) PV_12(S) PV_EL(12, S) PV_EL(13, S) PV_EL(14, S) PV_EL(15, S)
  PV_STEP(0)
  PV_STEP(1)
  PV_STEP(2)
  PV_12(3)
  {
    v4u f0 = P_FRAG(12, 3), f1 = P_FRAG(13, 3), f2 = P_FRAG(14, 3), f3 = P_FRAG(15, 3);
    PIN(f0); PIN(f1); PIN(f2); PIN(f3);
    SCHED_FENCE();
    acc = mfma16(f0, kb[124], acc); acc = mfma16(f1, kb[125], acc); acc = mfma16(f2, kb[126], acc); acc = mfma16(f3, kb[127], acc);
    PIN(acc); SCHED_FENCE();
  }
  if (!have) {
    if (w == CW) {
      int cnt = LDS_GET(L_Q + 4), now = NOW(), last = now;
#pragma nounroll
      for (int n = 0; n < 4096 && now < TP0; ++n) {
        __builtin_amdgcn_s_sleep(2);
        const int cn = LDS_GET(L_Q + 4);
        now = NOW();
        mg = max(mg, now - last);
        if (cn != cnt) { cnt = cn; last = now; }
      }
      const unsigned F = t_in + now + (NW * ((P1 - P0) / PG) - (cnt >> 4)) * TAUG / 10;
      if (lane == 0) __builtin_amdgcn_raw_buffer_store_b128(v4u{F, tagA, (unsigned)min(mg, 400) << 16 | (t_q & 0xffffu), tagB}, rws, b * 16, WS_REP, AUX_SC1);
      UNTIL(TP0 + TRD);
      v4u rp[4];
#pragma unroll
      for (int j = 0; j < 4; ++j) rp[j] = __builtin_amdgcn_raw_buffer_load_b128(rws, lo + j * 1024, WS_REP, AUX_SC1);
      float sum = 0.f, sq = 0.f, mx = 0.f, nr = 0.f, gs = 0.f, qt = 0.f;
#pragma unroll
      for (int j = 0; j < 4; ++j)
        if (rp[j][1] == tagA && rp[j][3] == tagB) {
          const float f = (float)(int)(rp[j][0] - t_in - TEB);
          sum += f; sq += f * f; mx = fmaxf(mx, f); nr += 1.f; gs += (float)(rp[j][2] >> 16); qt += (float)(short)(rp[j][2] - t_in);
        }
      sum = xrow_sum(row_sum16(sum)); sq = xrow_sum(row_sum16(sq)); nr = xrow_sum(row_sum16(nr)); mx = xrow_max(row_max16(mx));
      const float rn = __builtin_amdgcn_rcpf(fmaxf(nr, 1.f)), miss = 256.f - nr;
      gs = xrow_sum(row_sum16(gs)) * rn; qt = xrow_sum(row_sum16(qt)) * rn;
      const float mf = (sum + miss * mx) * (1.f / 256), sd = __builtin_amdgcn_sqrtf(fmaxf((sq + miss * mx * mx) * (1.f / 256) - mf * mf, 0.f));
      const float stall = fminf(fmaxf((gs - G0) * (1.f / (G1 - G0)), 0.f), 1.f) * HIC;
      const float dt = mf + (mf + (TEB - D0) - qt - stall) * (LAM * 0.01f) + (sd - SD0) * (MU * 0.01f) + CM;
      const int T = TEB + (int)fminf(fmaxf(dt, (float)-TLO), 800.f);
      __hip_atomic_store((unsigned*)&lds[L_Q], t_in + T, __ATOMIC_RELAXED, __HIP_MEMORY_SCOPE_WORKGROUP);
    }
#pragma nounroll
    for (int n = 0; n < POLL_MAX && LDS_GET(L_CNT) < NW; ++n) __builtin_amdgcn_s_sleep(2);
    WG_MAX();
  }
  {
    const float a0 = fold32(acc[0], acc[2]) * fw, a1 = fold32(acc[1], acc[3]) * fw;
    const unsigned hi = pack_bf16(a0, a1);
    LDS_AT(unsigned, L_A, a_aw) = hi;
    LDS_AT(unsigned, L_A + 32, a_aw) = pack_bf16(a0 - bf_lo(hi), a1 - bf_hi(hi));
  }
  if (w < 4) {
    UNTIL(LDS_GET(L_Q) - (int)t_in - 2 * REARLY);
    u2 = __builtin_amdgcn_raw_buffer_load_b128(rws, a_ml, 0, AUX_SC1);
  }
  WG_BARRIER();
  {
    v4u af[4];
#pragma unroll
    for (int kk = 0; kk < 4; ++kk) af[kk] = LDS_AT(v4u, L_A + 256 * kk, lo);
    SCHED_FENCE();
    f32x4 y[5];
    float s[5];
#pragma unroll
    for (int i = 0; i < 5; ++i) y[i] = mfma16(af[0], wo[i], f32x4{0.f, 0.f, 0.f, 0.f});
    SCHED_FENCE();
#pragma unroll
    for (int i = 0; i < 5; ++i) y[i] = mfma16(af[1], wo[5 + i], y[i]);
#define Y_FIN(i) SCHED_FENCE(); y[i] = mfma16(af[2], wo[10 + (i)], y[i]); y[i] = mfma16(af[3], wo[15 + (i)], y[i]); SCHED_FENCE();
#define Y_SUM(i) s[i] = (y[i][0] + y[i][2]) + (y[i][1] + y[i][3]);
    Y_FIN(4) Y_FIN(0) Y_SUM(4) Y_FIN(1) Y_SUM(0)
    const float ex = half_sum(s[4]), ey = dpp<0x128>(ex);
    Y_FIN(2) Y_SUM(1) Y_FIN(3) Y_SUM(2)
    const float t0 = fold16(fold32(s[0], s[1]), ex);
    SCHED_FENCE();
    Y_SUM(3)
    const float t1 = fold16(fold32(s[2], s[3]), ey);
    __builtin_amdgcn_raw_buffer_store_b128(v4u{__float_as_uint(t0), tagA, __float_as_uint(t1), tagB}, rws, a_st, s_st, AUX_SC1);
  }

  if (red < 0 && !keeper) return;
  {
#pragma nounroll
    for (int n = 0; n < POLL_MAX && (u2[1] != tagA || u2[3] != tagB); ++n) {
      asm volatile("" ::: "memory");
      u2 = __builtin_amdgcn_raw_buffer_load_b128(rws, a_ml, 0, AUX_SC1);
    }
    if (keeper) {
      WG_BARRIER();
      if (tid < 8) __builtin_amdgcn_raw_buffer_store_b128(v4u{tagA, tagB, tagA, tagB}, rws, lo, WS_EPOCH, AUX_SC1);
      return;
    }
    float cf;
    {
      const float mp = __uint_as_float(u2[0]), lp = __uint_as_float(u2[2]);
      float M = fmaxf(mp, dpp<0x128>(mp)); M = fmaxf(M, dpp<0x124>(M));
      const float f = __builtin_amdgcn_exp2f((mp - M) * LOG2E);
      float L = f * lp; L += dpp<0x128>(L); L += dpp<0x124>(L);
      cf = f * __builtin_amdgcn_rcpf(L);
      PIN(cf);
    }
    const int tq = LDS_GET(L_Q) - (int)t_in + TPOLL;
    UNTIL(tq);
    v4u u0, u1;
    int n = 0;
#pragma nounroll
    do {
      asm volatile("" ::: "memory");
      u0 = __builtin_amdgcn_raw_buffer_load_b128(rws, a_y, 0, AUX_SC1);
      u1 = __builtin_amdgcn_raw_buffer_load_b128(rws, a_y, 16, AUX_SC1);
    } while (++n < POLL_MAX && ((u0[1] ^ tagA) | (u0[3] ^ tagB) | (u1[1] ^ tagA) | (u1[3] ^ tagB)) != 0);
    float t = fold16(fold32(__uint_as_float(u0[0]) * cf, __uint_as_float(u0[2]) * cf),
                     fold32(__uint_as_float(u1[0]) * cf, __uint_as_float(u1[2]) * cf));
    t += dpp<0x128>(t); t += dpp<0x124>(t);
    LDS_AT(float, L_R, a_rw) = t;
    asm volatile("" ::: "memory");
    const int nd = __hip_atomic_fetch_add(&LDS_AT(int, L_CNT, a_cn), 1, __ATOMIC_RELAXED, __HIP_MEMORY_SCOPE_WORKGROUP);
    asm volatile("" ::: "memory");
    f32x4 s4 = LDS_AT(f32x4, L_R, a_rr) + LDS_AT(f32x4, L_R + 4, a_rr);
    PIN(s4);
    if (__builtin_amdgcn_readfirstlane(nd) == 2 * NW - 1) {
      asm volatile(".Lattn_pf_to:");
      __builtin_amdgcn_raw_buffer_store_b16((unsigned short)pack_bf16((s4[0] + s4[2]) + (s4[1] + s4[3]), 0.f), mk_rsrc(B.out, DM * 4), a_o, 0, 0);
    }
  }
}

extern "C" void kernel_launch(void* const* d_in, const int* in_sizes, int n_in,
                              void* d_out, int out_size, void* d_ws, size_t ws_size,
                              hipStream_t stream) {
  Params P;
  for (int i = 0; i < 2; ++i) {
    P.b[i].x = d_in[5 * i]; P.b[i].k = d_in[5 * i + 1]; P.b[i].v = d_in[5 * i + 2];
    P.b[i].wq = d_in[5 * i + 3]; P.b[i].wo = d_in[5 * i + 4];
    P.b[i].out = d_out; P.b[i].ws = d_ws; P.b[i].ws2 = d_ws;
  }
  hipLaunchKernelGGL(attn_decode_fused, dim3(256), dim3(NTHR), 0, stream, P);
}
```
